```python
import torch

def _softmax(x, axis=-1):
    return torch.softmax(x, dim=axis)
def _gelu(x):
    return torch.nn.functional.gelu(x, approximate="tanh")
def _silu(x):
    return torch.nn.functional.silu(x)
def _sigmoid(x):
    return torch.sigmoid(x)
def _layernorm(x, axis=-1, eps=1e-5):
    m = x.mean(dim=axis, keepdim=True)
    v = x.var(dim=axis, keepdim=True, unbiased=False)
    return (x - m) / torch.sqrt(v + eps)
def _scatter_add(tgt, idx, src, axis=0):
    return tgt.clone().index_add_(axis, idx, src)
def _segment_sum(src, seg, n):
    out = torch.zeros((n,) + src.shape[1:], dtype=src.dtype)
    return out.index_add_(0, seg, src)
def _pad(x, pw):
    flat = tuple(v for pair in reversed(pw) for v in pair)
    return torch.nn.functional.pad(x, flat)

kernel_name = 'standalone__groupnorm__sampling'

def setup_inputs():

    _w = (lambda a: (lambda t: t.to(torch.bfloat16) if t.is_floating_point() else t)(torch.as_tensor(a)))
    B = 32
    G = 32
    cpg = max(1, 1024 // G)
    C = G * cpg
    H, W = 32, 16
    return {
        "x": _w(abs((lambda *s: torch.randn(*s, dtype=torch.bfloat16) * 0.05)(B, C, H, W))),
        "g": _w((lambda *s: torch.randn(*s, dtype=torch.bfloat16) * 0.05)(C) + 1.0),
        "b": _w((lambda *s: torch.randn(*s, dtype=torch.bfloat16) * 0.4)(C)),
    }

def reference(x, g, b):
    G = 32
    B, C, H, W = x.shape[0], x.shape[1], x.shape[2], x.shape[3]
    xr = x.reshape(B, G, C // G, H, W)
    mu = xr.mean(axis=(2, 3, 4), keepdims=True)
    var = ((xr - mu) ** 2).mean(axis=(2, 3, 4), keepdims=True)
    xn = (xr - mu) / torch.sqrt(var + 1e-5)
    xn = xn.reshape(B, C, H, W)
    return xn * g[None, :, None, None] + b[None, :, None, None]
```

```cpp
#include <hip/hip_runtime.h>
#include <hip/hip_bf16.h>
#include <stdint.h>

typedef uint32_t u32x4 __attribute__((ext_vector_type(4)));
typedef float f32x2 __attribute__((ext_vector_type(2)));
typedef __bf16 bf16x2 __attribute__((ext_vector_type(2)));

#define LOAD_NT 0
#define STORE_NT 1

constexpr int kC = 1024;
constexpr int kG = 32;
constexpr int kCpg = 32;
constexpr int kHW = 512;
constexpr int kGroupElems = kCpg * kHW;

__device__ __forceinline__ float bf_lo(uint32_t v) { return __uint_as_float(v << 16); }
__device__ __forceinline__ float bf_hi(uint32_t v) { return __uint_as_float(v & 0xffff0000u); }

__device__ __forceinline__ uint32_t pack_bf16(float a, float b) {
    f32x2 v = {a, b};
    bf16x2 r = __builtin_convertvector(v, bf16x2);
    return __builtin_bit_cast(uint32_t, r);
}

template <int CTRL, int ROWMASK = 0xf>
__device__ __forceinline__ float dpp_add(float v) {
    int t = __builtin_amdgcn_update_dpp(0, __float_as_int(v), CTRL, ROWMASK, 0xf, true);
    return v + __int_as_float(t);
}

__device__ __forceinline__ float wave_sum(float v) {
    v = dpp_add<0xB1>(v);
    v = dpp_add<0x4E>(v);
    v = dpp_add<0x141>(v);
    v = dpp_add<0x140>(v);
    float a = __int_as_float(__builtin_amdgcn_readlane(__float_as_int(v), 0));
    float b = __int_as_float(__builtin_amdgcn_readlane(__float_as_int(v), 16));
    float c = __int_as_float(__builtin_amdgcn_readlane(__float_as_int(v), 32));
    float d = __int_as_float(__builtin_amdgcn_readlane(__float_as_int(v), 48));
    return (a + b) + (c + d);
}

__global__ __launch_bounds__(256) void gn_kernel(const u32x4* __restrict__ x,
                                                 const uint16_t* __restrict__ gamma,
                                                 const uint16_t* __restrict__ beta,
                                                 u32x4* __restrict__ out) {
    constexpr int NW = 4;
    constexpr int CPW = kCpg / NW;
    __shared__ f32x2 red[NW];
    const int grp = blockIdx.x;
    const int lane = threadIdx.x & 63;
    const int wave = __builtin_amdgcn_readfirstlane(threadIdx.x >> 6);
    const size_t base = (size_t)grp * (kGroupElems / 8) + wave * CPW * 64 + lane;

    u32x4 v[CPW];
#pragma unroll
    for (int i = 0; i < CPW; ++i) {
        v[i] = x[base + i * 64];
    }
    const int c0 = (grp & (kG - 1)) * kCpg + wave * CPW;
    const u32x4 gq = *reinterpret_cast<const u32x4*>(gamma + c0);
    const u32x4 bq = *reinterpret_cast<const u32x4*>(beta + c0);

    float f[CPW * 8];
    float s = 0.f;
#pragma unroll
    for (int i = 0; i < CPW; ++i) {
        float t = 0.f;
#pragma unroll
        for (int j = 0; j < 4; ++j) {
            f[i * 8 + 2 * j] = bf_lo(v[i][j]);
            f[i * 8 + 2 * j + 1] = bf_hi(v[i][j]);
            t += f[i * 8 + 2 * j] + f[i * 8 + 2 * j + 1];
        }
        s += t;
    }
    const float mw = wave_sum(s) * (1.0f / (CPW * kHW));
    float q = 0.f;
#pragma unroll
    for (int i = 0; i < CPW; ++i) {
        float t = 0.f;
#pragma unroll
        for (int j = 0; j < 8; ++j) {
            f[i * 8 + j] -= mw;
            t = fmaf(f[i * 8 + j], f[i * 8 + j], t);
        }
        q += t;
    }
    const float qw = wave_sum(q);
    if (lane == 0) red[wave] = f32x2{mw, qw};
    __syncthreads();
    f32x2 r[NW];
#pragma unroll
    for (int w = 0; w < NW; ++w) r[w] = red[w];
    float mu = 0.f, m2 = 0.f;
#pragma unroll
    for (int w = 0; w < NW; ++w) { mu += r[w].x; m2 += r[w].y; }
    mu *= (1.0f / NW);
    float dv = 0.f;
#pragma unroll
    for (int w = 0; w < NW; ++w) { float d = r[w].x - mu; dv = fmaf(d, d, dv); }
    const float var = m2 * (1.0f / kGroupElems) + dv * (1.0f / NW);
    const float rstd = 1.0f / sqrtf(var + 1e-5f);
    const float dm = mw - mu;
#pragma unroll
    for (int i = 0; i < CPW; ++i) {
        const float gc = (i & 1) ? bf_hi(gq[i >> 1]) : bf_lo(gq[i >> 1]);
        const float bc = (i & 1) ? bf_hi(bq[i >> 1]) : bf_lo(bq[i >> 1]);
        const float sc = rstd * gc;
        const float sh = fmaf(dm, sc, bc);
        u32x4 o;
#pragma unroll
        for (int j = 0; j < 4; ++j)
            o[j] = pack_bf16(fmaf(f[i * 8 + 2 * j], sc, sh), fmaf(f[i * 8 + 2 * j + 1], sc, sh));
        __builtin_nontemporal_store(o, &out[base + i * 64]);
    }
}

extern "C" void kernel_launch(void* const* d_in, const int* in_sizes, int n_in,
                              void* d_out, int out_size, void* d_ws, size_t ws_size,
                              hipStream_t stream) {
    const int ngroups = in_sizes[0] / kGroupElems;
    gn_kernel<<<ngroups, 256, 0, stream>>>((const u32x4*)d_in[0], (const uint16_t*)d_in[1],
                                           (const uint16_t*)d_in[2], (u32x4*)d_out);
}
```

```cpp
#include <hip/hip_runtime.h>
#include <stdint.h>

typedef uint32_t u32x4 __attribute__((ext_vector_type(4)));
typedef float f32x2 __attribute__((ext_vector_type(2)));
typedef float f32x4 __attribute__((ext_vector_type(4)));
typedef __bf16 bf16x2 __attribute__((ext_vector_type(2)));

#define LAUX 2
#define SAUX 16
#define EARLYG 3

constexpr int kG = 32, kCpg = 32, kHW = 512, kGroupElems = 16384;

__device__ __forceinline__ float bf_lo(uint32_t v) { return __uint_as_float(v << 16); }
__device__ __forceinline__ float bf_hi(uint32_t v) { return __uint_as_float(v & 0xffff0000u); }
__device__ __forceinline__ uint32_t pack_bf16(f32x2 v) {
    bf16x2 r = __builtin_convertvector(v, bf16x2);
    return __builtin_bit_cast(uint32_t, r);
}
template <int CTRL>
__device__ __forceinline__ float dpp_add(float v) {
    int t = __builtin_amdgcn_update_dpp(0, __float_as_int(v), CTRL, 0xf, 0xf, true);
    return v + __int_as_float(t);
}
__device__ __forceinline__ float wave_sum(float v) {
    v = dpp_add<0xB1>(v); v = dpp_add<0x4E>(v); v = dpp_add<0x141>(v); v = dpp_add<0x140>(v);
    float a = __int_as_float(__builtin_amdgcn_readlane(__float_as_int(v), 0));
    float b = __int_as_float(__builtin_amdgcn_readlane(__float_as_int(v), 16));
    float c = __int_as_float(__builtin_amdgcn_readlane(__float_as_int(v), 32));
    float d = __int_as_float(__builtin_amdgcn_readlane(__float_as_int(v), 48));
    return (a + b) + (c + d);
}

template <int LA, int SA, int EARLY>
__global__ __launch_bounds__(1024) __attribute__((aligned(4096))) void gn(const void* __restrict__ x, const uint16_t* __restrict__ gamma,
                                           const uint16_t* __restrict__ beta, void* __restrict__ out) {
    constexpr int CPW = 8, WPG = 4;
    __shared__ __attribute__((aligned(16))) f32x2 red[16];
    const int lane = threadIdx.x & 63;
    const int wave = __builtin_amdgcn_readfirstlane(threadIdx.x >> 6);
    const int gi = wave >> 2, wi = wave & 3;
    const int grp = blockIdx.x + gi * 256;
    const int woff = wi * 8192;
    __amdgpu_buffer_rsrc_t rx = __builtin_amdgcn_make_buffer_rsrc((char*)x + (size_t)grp * 32768 + woff, 0, 8192, 0x00020000);
    __amdgpu_buffer_rsrc_t ro = __builtin_amdgcn_make_buffer_rsrc((char*)out + (size_t)grp * 32768 + woff, 0, 8192, 0x00020000);
    u32x4 v[CPW];
#pragma unroll
    for (int i = 0; i < CPW; ++i) v[i] = __builtin_amdgcn_raw_buffer_load_b128(rx, lane * 16 + i * 1024, 0, LA);
    const int c0 = (grp & (kG - 1)) * kCpg + wi * CPW;
    u32x4 gq, bq;
    asm volatile("s_load_dwordx4 %0, %2, %4\n s_load_dwordx4 %1, %3, %4"
                 : "=&s"(gq), "=&s"(bq) : "s"(gamma), "s"(beta), "s"(c0 * 2), "s"(ro));
    typedef volatile __attribute__((address_space(3))) f32x2* lp2;
    typedef volatile __attribute__((address_space(3))) f32x4* lp4;
    const bool late = gi >= EARLY;
    if (EARLY < 4 && late) {
        if (lane == 0) *(lp2)&red[wave] = f32x2{0.f, -1.f};
        asm volatile("s_waitcnt lgkmcnt(0)\n s_barrier" ::: "memory");
    }

    f32x2 f[CPW * 4];
    f32x2 s = {0.f, 0.f};
#pragma unroll
    for (int i = 0; i < CPW; ++i) {
#pragma unroll
        for (int j = 0; j < 4; ++j) f[i * 4 + j] = f32x2{bf_lo(v[i][j]), bf_hi(v[i][j])};
        s += (f[i * 4] + f[i * 4 + 1]) + (f[i * 4 + 2] + f[i * 4 + 3]);
    }
    const float mw = wave_sum(s.x + s.y) * (1.0f / (CPW * kHW));
    f32x2 q0 = {0.f, 0.f}, q1 = {0.f, 0.f};
#pragma unroll
    for (int i = 0; i < CPW * 4; i += 2) {
        f[i] -= mw; f[i + 1] -= mw;
        q0 = __builtin_elementwise_fma(f[i], f[i], q0);
        q1 = __builtin_elementwise_fma(f[i + 1], f[i + 1], q1);
    }
    q0 += q1;
    const float qw = wave_sum(q0.x + q0.y);
    if (lane == 0) *(lp2)&red[wave] = f32x2{mw, qw};
    f32x2 r[WPG];
    asm volatile("s_waitcnt lgkmcnt(0)" : "+s"(gq), "+s"(bq) :: "memory");
    if (!late) asm volatile("s_barrier" ::: "memory");
    float gc[CPW], bc[CPW];
#pragma unroll
    for (int i = 0; i < CPW / 2; ++i) {
        gc[2 * i] = bf_lo(gq[i]); gc[2 * i + 1] = bf_hi(gq[i]);
        bc[2 * i] = bf_lo(bq[i]); bc[2 * i + 1] = bf_hi(bq[i]);
    }
    for (;;) {
        const f32x4 ra = *(lp4)&red[gi * WPG], rb = *(lp4)&red[gi * WPG + 2];
        r[0] = f32x2{ra.x, ra.y}; r[1] = f32x2{ra.z, ra.w}; r[2] = f32x2{rb.x, rb.y}; r[3] = f32x2{rb.z, rb.w};
        if (EARLY == 4) break;
        if (!(r[0].y < 0.f) && !(r[1].y < 0.f) && !(r[2].y < 0.f) && !(r[3].y < 0.f)) break;
        __builtin_amdgcn_s_sleep(1);
    }
    float mu = 0.f, m2 = 0.f, dv = 0.f;
#pragma unroll
    for (int w = 0; w < WPG; ++w) { mu += r[w].x; m2 += r[w].y; }
    mu *= (1.0f / WPG);
#pragma unroll
    for (int w = 0; w < WPG; ++w) { float d = r[w].x - mu; dv = fmaf(d, d, dv); }
    const float var = m2 * (1.0f / kGroupElems) + dv * (1.0f / WPG);
    const float rstd = __builtin_amdgcn_rsqf(var + 1e-5f);
    const float dm = mw - mu;
#pragma unroll
    for (int i = 0; i < CPW; ++i) {
        const float sc = rstd * gc[i];
        const float sh = fmaf(dm, sc, bc[i]);
        const f32x2 sc2 = {sc, sc}, sh2 = {sh, sh};
        u32x4 o;
#pragma unroll
        for (int j = 0; j < 4; ++j) o[j] = pack_bf16(__builtin_elementwise_fma(f[i * 4 + j], sc2, sh2));
        __builtin_amdgcn_raw_buffer_store_b128(o, ro, lane * 16 + i * 1024, 0, SA);
    }
}

extern "C" void kernel_launch(void* const* d_in, const int* in_sizes, int n_in,
                              void* d_out, int out_size, void* d_ws, size_t ws_size,
                              hipStream_t stream) {
    gn<LAUX, SAUX, EARLYG><<<256, 1024, 0, stream>>>(d_in[0], (const uint16_t*)d_in[1],
                                                     (const uint16_t*)d_in[2], d_out);
}
```
